```python
import torch

def _softmax(x, axis=-1):
    return torch.softmax(x, dim=axis)
def _gelu(x):
    return torch.nn.functional.gelu(x, approximate="tanh")
def _silu(x):
    return torch.nn.functional.silu(x)
def _sigmoid(x):
    return torch.sigmoid(x)
def _layernorm(x, axis=-1, eps=1e-5):
    m = x.mean(dim=axis, keepdim=True)
    v = x.var(dim=axis, keepdim=True, unbiased=False)
    return (x - m) / torch.sqrt(v + eps)
def _scatter_add(tgt, idx, src, axis=0):
    return tgt.clone().index_add_(axis, idx, src)
def _segment_sum(src, seg, n):
    out = torch.zeros((n,) + src.shape[1:], dtype=src.dtype)
    return out.index_add_(0, seg, src)
def _pad(x, pw):
    flat = tuple(v for pair in reversed(pw) for v in pair)
    return torch.nn.functional.pad(x, flat)

kernel_name = 'gated_mix__linear_kernel_hedgehog__mixer_s_mlp__prompt'

def setup_inputs():

    _w = (lambda a: (lambda t: t.to(torch.bfloat16) if t.is_floating_point() else t)(torch.as_tensor(a)))
    def _A_setup():
        B, S, D, H = 1, 2048, 1024, 32
        Dh = D // H
        return {
            "x": _w((lambda *s: torch.randn(*s, dtype=torch.bfloat16) * 0.05)(B, S, D)),
            "wqkv": _w((lambda *s: torch.randn(*s, dtype=torch.bfloat16) * 1.6)(D, 3 * D)),
            "w_phi": _w((lambda *s: torch.randn(*s, dtype=torch.bfloat16) * 1.6)(H, Dh, Dh) * (Dh ** -0.5)),
            "wo": _w((lambda *s: torch.randn(*s, dtype=torch.bfloat16) * 0.8)(D, D)),
        }
    _A_in = _A_setup()

    def _B_setup():
        B, S, D, F = 1, 2048, 1024, 3584
        return {
            "x":  _w((lambda *s: torch.randn(*s, dtype=torch.bfloat16) * 0.05)(B, S, D)),
            "w1": _w((lambda *s: torch.randn(*s, dtype=torch.bfloat16) * 3.2)(D, D) * (D ** -0.5)),
            "w2": _w((lambda *s: torch.randn(*s, dtype=torch.bfloat16) * 3.2)(D, D) * (D ** -0.5)),
            "w3": _w((lambda *s: torch.randn(*s, dtype=torch.bfloat16) * 3.2)(D, F) * (D ** -0.5)),
            "w4": _w((lambda *s: torch.randn(*s, dtype=torch.bfloat16) * 1.6)(F, D) * (F ** -0.5)),
        }
    _B_in = _B_setup()

    return {
        "A_x": _A_in["x"],
        "A_wqkv": _A_in["wqkv"],
        "A_w_phi": _A_in["w_phi"],
        "A_wo": _A_in["wo"],
        "B_x": _B_in["x"],
        "B_w1": _B_in["w1"],
        "B_w2": _B_in["w2"],
        "B_w3": _B_in["w3"],
        "B_w4": _B_in["w4"],
        "gate_w": _w((lambda *s: torch.randn(*s, dtype=torch.bfloat16) * 0.05)(1024) * 16.0),
    }

def reference(A_x, A_wqkv, A_w_phi, A_wo, B_x, B_w1, B_w2, B_w3, B_w4, gate_w):
    def _A_ref(x, wqkv, w_phi, wo):
        B, S, D = x.shape
        H = 32; Dh = D // H
        qkv = (lambda x, *d: x.permute(*d))((x @ wqkv).reshape(B, S, 3, H, Dh), 2, 0, 3, 1, 4)
        q, k, v = qkv[0], qkv[1], qkv[2]
        pq = torch.einsum("bhsd,hdf->bhsf", q, w_phi)
        pk = torch.einsum("bhsd,hdf->bhsf", k, w_phi)
        phi_q = torch.concatenate([_softmax(pq, axis=-1), _softmax(-pq, axis=-1)], axis=-1)
        phi_k = torch.concatenate([_softmax(pk, axis=-1), _softmax(-pk, axis=-1)], axis=-1)
        kv = torch.einsum("bhsf,bhsd->bhfd", phi_k, v)
        z  = torch.einsum("bhsf->bhf", phi_k)
        num = torch.einsum("bhqf,bhfd->bhqd", phi_q, kv)
        den = torch.einsum("bhqf,bhf->bhq", phi_q, z)[..., None] + 1e-6
        o = num / den
        return (lambda x, *d: x.permute(*d))(o, 0, 2, 1, 3).reshape(B, S, D) @ wo

    def _B_ref(x, w1, w2, w3, w4):
        B, S, D = x.shape
        h = _gelu(_layernorm(x, axis=-1) @ w1)
        c = D // 4
        g0 = _pad(h[:, :, 0*c:1*c], ((0, 0), (1, 0), (0, 0)))[:, :S, :]
        g1 = _pad(h[:, :, 1*c:2*c], ((0, 0), (0, 1), (0, 0)))[:, 1:, :]
        g2 = _pad(h[:, :, 2*c:3*c], ((0, 0), (2, 0), (0, 0)))[:, :S, :]
        g3 = _pad(h[:, :, 3*c:   ], ((0, 0), (0, 2), (0, 0)))[:, 2:, :]
        hs = torch.concatenate([g0, g1, g2, g3], axis=-1) @ w2
        x1 = x + hs
        h2 = _gelu(_layernorm(x1, axis=-1) @ w3) @ w4
        return x1 + h2

    x = A_x
    g = _sigmoid((x * gate_w).sum(-1, keepdims=True))
    a = _A_ref(x, A_wqkv, A_w_phi, A_wo)
    b = _B_ref(x, B_w1, B_w2, B_w3, B_w4)
    return g * a + (1 - g) * b
```

```cpp
#include <hip/hip_runtime.h>
#include <hip/hip_bf16.h>
#include <stdint.h>

constexpr int S_ = 2048, D_ = 1024, H_ = 32, DH = 32, F_ = 3584;
constexpr int NQKV = 3 * D_;

typedef unsigned short u16;
typedef __attribute__((ext_vector_type(8))) short bf16x8;
typedef __attribute__((ext_vector_type(4))) short bf16x4;
typedef __attribute__((ext_vector_type(16))) float f32x16;
typedef __attribute__((ext_vector_type(4))) float f32x4;
typedef __attribute__((ext_vector_type(4))) unsigned int u32x4;
typedef __attribute__((ext_vector_type(2))) unsigned int u32x2;

__device__ __forceinline__ float bf2f(u16 v) { return __uint_as_float(((unsigned)v) << 16); }
__device__ __forceinline__ u16 f2bf(float f) {
  __hip_bfloat16 b = __float2bfloat16(f);
  return *reinterpret_cast<u16*>(&b);
}
__device__ __forceinline__ float rbf(float f) { return bf2f(f2bf(f)); }
__device__ __forceinline__ float gelu_f(float x) {
  const float k0 = 0.7978845608028654f, k1 = 0.044715f;
  float inner = k0 * (x + k1 * x * x * x);
  return 0.5f * x * (1.0f + tanhf(inner));
}

constexpr size_t WS_QKV   = 0;
constexpr size_t WS_H     = WS_QKV + (size_t)S_ * NQKV * 2;
constexpr size_t WS_X1    = WS_H + (size_t)(S_ + 4) * D_ * 2;
constexpr size_t WS_H3    = WS_X1 + (size_t)S_ * D_ * 2;
constexpr size_t WS_O     = WS_H3 + (size_t)S_ * F_ * 2;
constexpr size_t WS_XSTAT = WS_O + (size_t)S_ * D_ * 2;
constexpr size_t WS_GATE  = WS_XSTAT + (size_t)S_ * 2 * 4;
constexpr size_t WS_CS1   = WS_GATE + (size_t)S_ * 4;
constexpr size_t WS_CS3   = WS_CS1 + (size_t)D_ * 4;
constexpr size_t WS_PSTAT = WS_CS3 + (size_t)F_ * 4;
constexpr size_t WS_KVP   = WS_PSTAT + (size_t)S_ * 16 * 2 * 4;
constexpr size_t WS_ZP    = WS_KVP + (size_t)H_ * 16 * 64 * 32 * 4;
constexpr size_t WS_END   = WS_ZP + (size_t)H_ * 8 * 64 * 4;

__global__ __launch_bounds__(256) void prep_kernel(const u16* __restrict__ x, const u16* __restrict__ gate_w,
                                                   const u16* __restrict__ w1, const u16* __restrict__ w3,
                                                   float* __restrict__ xstat, float* __restrict__ gate,
                                                   float* __restrict__ cs1, float* __restrict__ cs3,
                                                   u16* __restrict__ hbuf) {
  __shared__ float red[32][65];
  const int tid = threadIdx.x, lane = tid & 63, wid = tid >> 6;
  int b = blockIdx.x;
  if (b < S_ / 4) {
    int row = b * 4 + wid;
    const u16* xr = x + (size_t)row * D_;
    float s = 0.f, ss = 0.f, gd = 0.f;
#pragma unroll
    for (int i = 0; i < 2; ++i) {
      int c = (i * 64 + lane) * 8;
      bf16x8 v = *reinterpret_cast<const bf16x8*>(xr + c);
      bf16x8 g = *reinterpret_cast<const bf16x8*>(gate_w + c);
#pragma unroll
      for (int j = 0; j < 8; ++j) {
        float f = bf2f((u16)v[j]);
        s += f; ss += f * f;
        gd += rbf(f * bf2f((u16)g[j]));
      }
    }
#pragma unroll
    for (int o = 32; o > 0; o >>= 1) {
      s += __shfl_xor(s, o); ss += __shfl_xor(ss, o); gd += __shfl_xor(gd, o);
    }
    if (lane == 0) {
      float mean = s * (1.0f / D_);
      float var = ss * (1.0f / D_) - mean * mean;
      var = fmaxf(var, 0.f);
      xstat[row * 2] = mean;
      xstat[row * 2 + 1] = rsqrtf(var + 1e-5f);
      float gs = rbf(gd);
      gate[row] = rbf(1.0f / (1.0f + __expf(-gs)));
    }
    return;
  }
  b -= S_ / 4;
  if (b < 16 + 56) {
    const u16* w; float* out; int ldw; int n0;
    if (b < 16) { w = w1; out = cs1; ldw = D_; n0 = b * 64; }
    else { w = w3; out = cs3; ldw = F_; n0 = (b - 16) * 64; }
    int ch = tid & 7, kg = tid >> 3;
    float acc[8] = {0, 0, 0, 0, 0, 0, 0, 0};
    for (int r = 0; r < 32; ++r) {
      bf16x8 v = *reinterpret_cast<const bf16x8*>(w + (size_t)(kg * 32 + r) * ldw + n0 + ch * 8);
#pragma unroll
      for (int j = 0; j < 8; ++j) acc[j] += bf2f((u16)v[j]);
    }
#pragma unroll
    for (int j = 0; j < 8; ++j) red[kg][ch * 8 + j] = acc[j];
    __syncthreads();
    if (tid < 64) {
      float s = 0.f;
      for (int k = 0; k < 32; ++k) s += red[k][tid];
      out[n0 + tid] = s;
    }
    return;
  }
  b -= 72;
  {
    int idx = tid * 8;
    u32x4 z = {0, 0, 0, 0};
    if (b == 0) *reinterpret_cast<u32x4*>(hbuf + idx) = z;
    else *reinterpret_cast<u32x4*>(hbuf + (size_t)(S_ + 2) * D_ + idx) = z;
  }
}

constexpr int BK = 64;
constexpr int NSTAGE = 3;

template <int BN> __device__ __forceinline__ int bswz(int row) {
  if constexpr (BN == 128) return ((row & 3) << 2) | ((row >> 2) & 3);
  else return ((row >> 1) & 1) << 2;
}

template <int BM, int BN, int WM, int WN>
struct Gemm {
  static constexpr int NW = WM * WN;
  static constexpr int TM = BM / WM / 32;
  static constexpr int TN = BN / WN / 32;
  static constexpr int A_BYTES = BM * BK * 2;
  static constexpr int B_BYTES = BK * BN * 2;
  static constexpr int STAGE_BYTES = A_BYTES + B_BYTES;
  static constexpr int APW = BM / 8 / NW;
  static constexpr int BPW = BN / 8 / NW;
  static constexpr int LPS = APW + BPW;

  template <bool SHIFT>
  static __device__ __forceinline__ void stage(char* lds, const u16* A, int lda, int m0, const u16* B, int ldb,
                                               int n0, int k0, int wid, int lane) {
    int shift = 0;
    if constexpr (SHIFT) {
      int q = k0 >> 8;
      shift = (q == 0) ? -1 : (q == 1) ? 1 : (q == 2) ? -2 : 2;
    }
#pragma unroll
    for (int j = 0; j < APW; ++j) {
      int ia = wid * APW + j;
      int row = 8 * ia + (lane >> 3);
      int c = (lane & 7) ^ ((row >> 1) & 7);
      const u16* src = A + (long)(m0 + row + shift) * lda + k0 + c * 8;
      __builtin_amdgcn_global_load_lds((const unsigned*)src, (__attribute__((address_space(3))) unsigned*)(lds + ia * 1024), 16, 0, 0);
    }
#pragma unroll
    for (int j = 0; j < BPW; ++j) {
      int ib = wid * BPW + j;
      constexpr int CPR = BN / 8;
      constexpr int RPI = 64 / CPR;
      int row = RPI * ib + lane / CPR;
      int c = (lane % CPR) ^ bswz<BN>(row);
      const u16* src = B + (long)(k0 + row) * ldb + n0 + c * 8;
      __builtin_amdgcn_global_load_lds((const unsigned*)src, (__attribute__((address_space(3))) unsigned*)(lds + A_BYTES + ib * 1024), 16, 0, 0);
    }
  }

  static __device__ __forceinline__ void compute(const char* lds, f32x16 (&acc)[TM][TN], int wm, int wn, int lane) {
    const int r = lane & 31, h = lane >> 5;
    const int blk = (lane >> 4) & 1, q = (lane & 15) >> 2, p = lane & 3;
#pragma unroll
    for (int s = 0; s < BK / 16; ++s) {
      bf16x8 af[TM], bfr[TN];
#pragma unroll
      for (int tm = 0; tm < TM; ++tm) {
        int row = wm * (TM * 32) + tm * 32 + r;
        af[tm] = *reinterpret_cast<const bf16x8*>(lds + row * 128 + (((2 * s + h) ^ ((row >> 1) & 7)) << 4));
      }
#pragma unroll
      for (int tn = 0; tn < TN; ++tn) {
        bf16x4 lo, hi;
        {
          int krow = 16 * s + 8 * h + q;
          int ch = (wn * (TN * 32) + tn * 32) / 8 + 2 * blk + (p >> 1);
          const char* a0 = lds + A_BYTES + krow * (BN * 2) + ((ch ^ bswz<BN>(krow)) << 4) + 8 * (p & 1);
          lo = __builtin_amdgcn_ds_read_tr16_b64_v4i16((__attribute__((address_space(3))) bf16x4*)a0);
          krow += 4;
          const char* a1 = lds + A_BYTES + krow * (BN * 2) + ((ch ^ bswz<BN>(krow)) << 4) + 8 * (p & 1);
          hi = __builtin_amdgcn_ds_read_tr16_b64_v4i16((__attribute__((address_space(3))) bf16x4*)a1);
        }
        bfr[tn] = __builtin_shufflevector(lo, hi, 0, 1, 2, 3, 4, 5, 6, 7);
      }
#pragma unroll
      for (int tm = 0; tm < TM; ++tm)
#pragma unroll
        for (int tn = 0; tn < TN; ++tn)
          acc[tm][tn] = __builtin_amdgcn_mfma_f32_32x32x16_bf16(af[tm], bfr[tn], acc[tm][tn], 0, 0, 0);
    }
  }

  template <bool SHIFT>
  static __device__ __forceinline__ void mainloop(char* lds, f32x16 (&acc)[TM][TN], const u16* A, int lda, int m0,
                                                  const u16* B, int ldb, int n0, int K, int wid, int lane) {
    const int wm = wid / WN, wn = wid % WN;
    const int nt = K / BK;
    stage<SHIFT>(lds, A, lda, m0, B, ldb, n0, 0, wid, lane);
    stage<SHIFT>(lds + STAGE_BYTES, A, lda, m0, B, ldb, n0, BK, wid, lane);
    int rb = 0, wb = 2;
    for (int t = 0; t < nt; ++t) {
      if (t + 1 < nt) asm volatile("s_waitcnt vmcnt(%0)" ::"n"(LPS) : "memory");
      else asm volatile("s_waitcnt vmcnt(0)" ::: "memory");
      __builtin_amdgcn_s_barrier();
      if (t + 2 < nt) stage<SHIFT>(lds + wb * STAGE_BYTES, A, lda, m0, B, ldb, n0, (t + 2) * BK, wid, lane);
      compute(lds + rb * STAGE_BYTES, acc, wm, wn, lane);
      rb = (rb == NSTAGE - 1) ? 0 : rb + 1;
      wb = (wb == NSTAGE - 1) ? 0 : wb + 1;
    }
    __builtin_amdgcn_s_barrier();
  }
};

using G1 = Gemm<256, 128, 4, 2>;
constexpr int EP_LD = 128 + 8;

__global__ __launch_bounds__(512, 2) void gemm1_kernel(const u16* __restrict__ x, const u16* __restrict__ wqkv,
                                                      const u16* __restrict__ w1, const float* __restrict__ xstat,
                                                      const float* __restrict__ cs1, u16* __restrict__ qkv,
                                                      u16* __restrict__ hbuf) {
  extern __shared__ __attribute__((aligned(16))) char lds[];
  const int tid = threadIdx.x, lane = tid & 63, wid = tid >> 6;
  const int bid = blockIdx.x;
  const int mt = bid & 7, ntile = bid >> 3;
  const int m0 = mt * 256;
  const bool isw1 = ntile >= 24;
  const int n0 = isw1 ? (ntile - 24) * 128 : ntile * 128;
  f32x16 acc[G1::TM][G1::TN];
#pragma unroll
  for (int i = 0; i < G1::TM; ++i)
#pragma unroll
    for (int j = 0; j < G1::TN; ++j) acc[i][j] = (f32x16)(0.f);
  G1::mainloop<false>(lds, acc, x, D_, m0, isw1 ? w1 : wqkv, isw1 ? D_ : NQKV, n0, D_, wid, lane);

  u16* tile = reinterpret_cast<u16*>(lds);
  const int wm = wid / 2, wn = wid % 2;
#pragma unroll
  for (int tm = 0; tm < G1::TM; ++tm)
#pragma unroll
    for (int tn = 0; tn < G1::TN; ++tn) {
      int col = wn * 64 + tn * 32 + (lane & 31);
      float csum = isw1 ? cs1[n0 + col] : 0.f;
#pragma unroll
      for (int rg = 0; rg < 16; ++rg) {
        int row = wm * 64 + tm * 32 + (rg & 3) + 8 * (rg >> 2) + 4 * (lane >> 5);
        float v = acc[tm][tn][rg];
        if (isw1) {
          float mean = xstat[(m0 + row) * 2], rstd = xstat[(m0 + row) * 2 + 1];
          v = gelu_f(rbf(rstd * (v - mean * csum)));
        }
        tile[row * EP_LD + col] = f2bf(v);
      }
    }
  __syncthreads();
  u16* dst = isw1 ? (hbuf + (size_t)2 * D_) : qkv;
  const int ldd = isw1 ? D_ : NQKV;
  for (int i = tid; i < 256 * 16; i += 512) {
    int row = i >> 4, ch = i & 15;
    u32x4 v = *reinterpret_cast<const u32x4*>(tile + row * EP_LD + ch * 8);
    *reinterpret_cast<u32x4*>(dst + (size_t)(m0 + row) * ldd + n0 + ch * 8) = v;
  }
}

using G2 = Gemm<128, 64, 2, 2>;
constexpr int EP2_LD = 64 + 8;

__global__ __launch_bounds__(256, 2) void gemm2_kernel(const u16* __restrict__ hbuf, const u16* __restrict__ w2,
                                                      const u16* __restrict__ x, u16* __restrict__ x1,
                                                      float* __restrict__ pstat) {
  extern __shared__ __attribute__((aligned(16))) char lds[];
  const int tid = threadIdx.x, lane = tid & 63, wid = tid >> 6;
  const int bid = blockIdx.x;
  const int mt = bid & 15, ntile = bid >> 4;
  const int m0 = mt * 128, n0 = ntile * 64;
  f32x16 acc[G2::TM][G2::TN];
#pragma unroll
  for (int i = 0; i < G2::TM; ++i)
#pragma unroll
    for (int j = 0; j < G2::TN; ++j) acc[i][j] = (f32x16)(0.f);
  G2::mainloop<true>(lds, acc, hbuf + (size_t)2 * D_, D_, m0, w2, D_, n0, D_, wid, lane);
  u16* tile = reinterpret_cast<u16*>(lds);
  const int wm = wid / 2, wn = wid % 2;
#pragma unroll
  for (int tm = 0; tm < G2::TM; ++tm)
#pragma unroll
    for (int tn = 0; tn < G2::TN; ++tn) {
      int col = wn * 32 + tn * 32 + (lane & 31);
#pragma unroll
      for (int rg = 0; rg < 16; ++rg) {
        int row = wm * 64 + tm * 32 + (rg & 3) + 8 * (rg >> 2) + 4 * (lane >> 5);
        tile[row * EP2_LD + col] = f2bf(acc[tm][tn][rg]);
      }
    }
  __syncthreads();
  for (int i = tid; i < 128 * 8; i += 256) {
    int row = i >> 3, ch = i & 7;
    bf16x8 hv = *reinterpret_cast<const bf16x8*>(tile + row * EP2_LD + ch * 8);
    bf16x8 xv = *reinterpret_cast<const bf16x8*>(x + (size_t)(m0 + row) * D_ + n0 + ch * 8);
    bf16x8 ov;
    float s = 0.f, ss = 0.f;
#pragma unroll
    for (int j = 0; j < 8; ++j) {
      u16 o = f2bf(bf2f((u16)xv[j]) + bf2f((u16)hv[j]));
      float f = bf2f(o);
      s += f; ss += f * f;
      ov[j] = (short)o;
    }
    *reinterpret_cast<bf16x8*>(x1 + (size_t)(m0 + row) * D_ + n0 + ch * 8) = ov;
#pragma unroll
    for (int o = 4; o > 0; o >>= 1) { s += __shfl_xor(s, o); ss += __shfl_xor(ss, o); }
    if (ch == 0) {
      pstat[((m0 + row) * 16 + ntile) * 2] = s;
      pstat[((m0 + row) * 16 + ntile) * 2 + 1] = ss;
    }
  }
}

__global__ __launch_bounds__(512, 2) void gemm3_kernel(const u16* __restrict__ x1, const u16* __restrict__ w3,
                                                      const float* __restrict__ pstat, const float* __restrict__ cs3,
                                                      u16* __restrict__ h3) {
  extern __shared__ __attribute__((aligned(16))) char lds[];
  const int tid = threadIdx.x, lane = tid & 63, wid = tid >> 6;
  const int bid = blockIdx.x;
  const int mt = bid & 7, ntile = bid >> 3;
  const int m0 = mt * 256, n0 = ntile * 128;
  float* rstat = reinterpret_cast<float*>(lds + NSTAGE * G1::STAGE_BYTES);
  if (tid < 256) {
    float s = 0.f, ss = 0.f;
    const float* p = pstat + (size_t)(m0 + tid) * 32;
    for (int i = 0; i < 16; ++i) { s += p[2 * i]; ss += p[2 * i + 1]; }
    float mean = s * (1.0f / D_);
    float var = fmaxf(ss * (1.0f / D_) - mean * mean, 0.f);
    rstat[tid * 2] = mean;
    rstat[tid * 2 + 1] = rsqrtf(var + 1e-5f);
  }
  f32x16 acc[G1::TM][G1::TN];
#pragma unroll
  for (int i = 0; i < G1::TM; ++i)
#pragma unroll
    for (int j = 0; j < G1::TN; ++j) acc[i][j] = (f32x16)(0.f);
  G1::mainloop<false>(lds, acc, x1, D_, m0, w3, F_, n0, D_, wid, lane);
  u16* tile = reinterpret_cast<u16*>(lds);
  const int wm = wid / 2, wn = wid % 2;
#pragma unroll
  for (int tm = 0; tm < G1::TM; ++tm)
#pragma unroll
    for (int tn = 0; tn < G1::TN; ++tn) {
      int col = wn * 64 + tn * 32 + (lane & 31);
      float csum = cs3[n0 + col];
#pragma unroll
      for (int rg = 0; rg < 16; ++rg) {
        int row = wm * 64 + tm * 32 + (rg & 3) + 8 * (rg >> 2) + 4 * (lane >> 5);
        float mean = rstat[row * 2], rstd = rstat[row * 2 + 1];
        float v = gelu_f(rbf(rstd * (acc[tm][tn][rg] - mean * csum)));
        tile[row * EP_LD + col] = f2bf(v);
      }
    }
  __syncthreads();
  for (int i = tid; i < 256 * 16; i += 512) {
    int row = i >> 4, ch = i & 15;
    u32x4 v = *reinterpret_cast<const u32x4*>(tile + row * EP_LD + ch * 8);
    *reinterpret_cast<u32x4*>(h3 + (size_t)(m0 + row) * F_ + n0 + ch * 8) = v;
  }
}

__global__ __launch_bounds__(256, 2) void gemm4_kernel(const u16* __restrict__ o, const u16* __restrict__ wo,
                                                      const u16* __restrict__ h3, const u16* __restrict__ w4,
                                                      const u16* __restrict__ x1, const float* __restrict__ gate,
                                                      u16* __restrict__ out) {
  extern __shared__ __attribute__((aligned(16))) char lds[];
  const int tid = threadIdx.x, lane = tid & 63, wid = tid >> 6;
  const int bid = blockIdx.x;
  const int mt = bid & 15, ntile = bid >> 4;
  const int m0 = mt * 128, n0 = ntile * 64;
  f32x16 acc[G2::TM][G2::TN], acc2[G2::TM][G2::TN];
#pragma unroll
  for (int i = 0; i < G2::TM; ++i)
#pragma unroll
    for (int j = 0; j < G2::TN; ++j) { acc[i][j] = (f32x16)(0.f); acc2[i][j] = (f32x16)(0.f); }
  G2::mainloop<false>(lds, acc, o, D_, m0, wo, D_, n0, D_, wid, lane);
  G2::mainloop<false>(lds, acc2, h3, F_, m0, w4, D_, n0, F_, wid, lane);
  u16* tileA = reinterpret_cast<u16*>(lds);
  u16* tileB = tileA + 128 * EP2_LD;
  const int wm = wid / 2, wn = wid % 2;
#pragma unroll
  for (int tm = 0; tm < G2::TM; ++tm)
#pragma unroll
    for (int tn = 0; tn < G2::TN; ++tn) {
      int col = wn * 32 + tn * 32 + (lane & 31);
#pragma unroll
      for (int rg = 0; rg < 16; ++rg) {
        int row = wm * 64 + tm * 32 + (rg & 3) + 8 * (rg >> 2) + 4 * (lane >> 5);
        tileA[row * EP2_LD + col] = f2bf(acc[tm][tn][rg]);
        tileB[row * EP2_LD + col] = f2bf(acc2[tm][tn][rg]);
      }
    }
  __syncthreads();
  for (int i = tid; i < 128 * 8; i += 256) {
    int row = i >> 3, ch = i & 7;
    bf16x8 av = *reinterpret_cast<const bf16x8*>(tileA + row * EP2_LD + ch * 8);
    bf16x8 hv = *reinterpret_cast<const bf16x8*>(tileB + row * EP2_LD + ch * 8);
    bf16x8 xv = *reinterpret_cast<const bf16x8*>(x1 + (size_t)(m0 + row) * D_ + n0 + ch * 8);
    float g = gate[m0 + row];
    float omg = rbf(1.0f - g);
    bf16x8 ov;
#pragma unroll
    for (int j = 0; j < 8; ++j) {
      float bb = rbf(bf2f((u16)xv[j]) + bf2f((u16)hv[j]));
      float t1 = rbf(g * bf2f((u16)av[j]));
      float t2 = rbf(omg * bb);
      ov[j] = (short)f2bf(t1 + t2);
    }
    *reinterpret_cast<bf16x8*>(out + (size_t)(m0 + row) * D_ + n0 + ch * 8) = ov;
  }
}

__device__ __forceinline__ void phi_tile(const u16* __restrict__ rows  , int ld,
                                         const bf16x8 (&wA)[2], int lane, float (&pp)[16], float (&pn)[16]) {
  const int r = lane & 31, h = lane >> 5;
  f32x16 d = (f32x16)(0.f);
#pragma unroll
  for (int ks = 0; ks < 2; ++ks) {
    bf16x8 b = *reinterpret_cast<const bf16x8*>(rows + (size_t)r * ld + ks * 16 + h * 8);
    d = __builtin_amdgcn_mfma_f32_32x32x16_bf16(wA[ks], b, d, 0, 0, 0);
  }
  float mx = -INFINITY, mn = INFINITY;
#pragma unroll
  for (int i = 0; i < 16; ++i) { float v = rbf(d[i]); d[i] = v; mx = fmaxf(mx, v); mn = fminf(mn, v); }
  mx = fmaxf(mx, __shfl_xor(mx, 32)); mn = fminf(mn, __shfl_xor(mn, 32));
  float sp = 0.f, sn = 0.f;
#pragma unroll
  for (int i = 0; i < 16; ++i) {
    pp[i] = __expf(d[i] - mx); pn[i] = __expf(mn - d[i]);
    sp += pp[i]; sn += pn[i];
  }
  sp += __shfl_xor(sp, 32); sn += __shfl_xor(sn, 32);
  float ip = 1.0f / sp, in_ = 1.0f / sn;
#pragma unroll
  for (int i = 0; i < 16; ++i) { pp[i] = rbf(pp[i] * ip); pn[i] = rbf(pn[i] * in_); }
}

__device__ __forceinline__ void load_wphiT(const u16* __restrict__ wphi, int head, int lane, bf16x8 (&wA)[2]) {
  const int r = lane & 31, h = lane >> 5;
#pragma unroll
  for (int ks = 0; ks < 2; ++ks)
#pragma unroll
    for (int j = 0; j < 8; ++j) wA[ks][j] = (short)wphi[(size_t)head * 1024 + (16 * ks + 8 * h + j) * 32 + r];
}

constexpr int PHI_LD = 256 + 8;
__global__ __launch_bounds__(256) void attn_kv_kernel(const u16* __restrict__ qkv, const u16* __restrict__ wphi,
                                                      float* __restrict__ kvp, float* __restrict__ zp) {
  __shared__ __attribute__((aligned(16))) u16 phi[64 * PHI_LD];
  __shared__ __attribute__((aligned(16))) u16 vT[32 * PHI_LD];
  const int tid = threadIdx.x, lane = tid & 63, wid = tid >> 6;
  const int head = blockIdx.x >> 3, sp = blockIdx.x & 7;
  const int s0 = sp * 256;
  bf16x8 wA[2];
  load_wphiT(wphi, head, lane, wA);
  const int h = lane >> 5;
#pragma unroll
  for (int t = 0; t < 2; ++t) {
    int sl = wid * 64 + t * 32;
    float pp[16], pn[16];
    phi_tile(qkv + (size_t)(s0 + sl) * NQKV + D_ + head * 32, NQKV, wA, lane, pp, pn);
#pragma unroll
    for (int i = 0; i < 16; ++i) {
      int f = (i & 3) + 8 * (i >> 2) + 4 * h;
      phi[f * PHI_LD + sl + (lane & 31)] = f2bf(pp[i]);
      phi[(32 + f) * PHI_LD + sl + (lane & 31)] = f2bf(pn[i]);
    }
  }
  {
    const u16* vr = qkv + (size_t)(s0 + tid) * NQKV + 2 * D_ + head * 32;
#pragma unroll
    for (int c = 0; c < 4; ++c) {
      bf16x8 v = *reinterpret_cast<const bf16x8*>(vr + c * 8);
#pragma unroll
      for (int j = 0; j < 8; ++j) vT[(c * 8 + j) * PHI_LD + tid] = (u16)v[j];
    }
  }
  __syncthreads();
  {
    const int ft = wid & 1, sh = wid >> 1;
    const int r = lane & 31;
    f32x16 d = (f32x16)(0.f);
#pragma unroll
    for (int ks = 0; ks < 8; ++ks) {
      int sb = sh * 128 + ks * 16 + h * 8;
      bf16x8 a = *reinterpret_cast<const bf16x8*>(phi + (ft * 32 + r) * PHI_LD + sb);
      bf16x8 b = *reinterpret_cast<const bf16x8*>(vT + r * PHI_LD + sb);
      d = __builtin_amdgcn_mfma_f32_32x32x16_bf16(a, b, d, 0, 0, 0);
    }
    float* dst = kvp + ((size_t)(head * 16 + sp * 2 + sh) * 64 + ft * 32) * 32;
#pragma unroll
    for (int i = 0; i < 16; ++i) {
      int f = (i & 3) + 8 * (i >> 2) + 4 * h;
      dst[f * 32 + r] = d[i];
    }
  }
  if (tid < 64) {
    float s = 0.f;
    for (int i = 0; i < 256; i += 8) {
      bf16x8 v = *reinterpret_cast<const bf16x8*>(phi + tid * PHI_LD + i);
#pragma unroll
      for (int j = 0; j < 8; ++j) s += bf2f((u16)v[j]);
    }
    zp[(head * 8 + sp) * 64 + tid] = s;
  }
}

constexpr int PQ_LD = 64 + 8;
__global__ __launch_bounds__(256) void attn_out_kernel(const u16* __restrict__ qkv, const u16* __restrict__ wphi,
                                                       const float* __restrict__ kvp, const float* __restrict__ zp,
                                                       u16* __restrict__ o) {
  __shared__ __attribute__((aligned(16))) u16 kvT[32 * PQ_LD];
  __shared__ float zs[64];
  __shared__ __attribute__((aligned(16))) u16 pq[4][32 * PQ_LD];
  const int tid = threadIdx.x, lane = tid & 63, wid = tid >> 6;
  const int head = blockIdx.x >> 3, sb = blockIdx.x & 7;
  const int s0 = sb * 256;
  for (int i = tid; i < 64 * 32; i += 256) {
    float s = 0.f;
    for (int p = 0; p < 16; ++p) s += kvp[(size_t)(head * 16 + p) * 2048 + i];
    int f = i >> 5, d = i & 31;
    kvT[d * PQ_LD + f] = f2bf(s);
  }
  if (tid < 64) {
    float s = 0.f;
    for (int p = 0; p < 8; ++p) s += zp[(head * 8 + p) * 64 + tid];
    zs[tid] = rbf(s);
  }
  bf16x8 wA[2];
  load_wphiT(wphi, head, lane, wA);
  __syncthreads();
  const int h = lane >> 5, r = lane & 31;
#pragma unroll
  for (int t = 0; t < 2; ++t) {
    int sl = wid * 64 + t * 32;
    float pp[16], pn[16];
    phi_tile(qkv + (size_t)(s0 + sl) * NQKV + head * 32, NQKV, wA, lane, pp, pn);
    float den = 0.f;
#pragma unroll
    for (int g = 0; g < 4; ++g) {
      int f = 8 * g + 4 * h;
      bf16x4 a, b;
#pragma unroll
      for (int j = 0; j < 4; ++j) {
        a[j] = (short)f2bf(pp[4 * g + j]); b[j] = (short)f2bf(pn[4 * g + j]);
        den += pp[4 * g + j] * zs[f + j] + pn[4 * g + j] * zs[32 + f + j];
      }
      *reinterpret_cast<bf16x4*>(&pq[wid][r * PQ_LD + f]) = a;
      *reinterpret_cast<bf16x4*>(&pq[wid][r * PQ_LD + 32 + f]) = b;
    }
    den += __shfl_xor(den, 32);
    den = rbf(rbf(den) + 1e-6f);
    __builtin_amdgcn_s_waitcnt(0xC07F);
    __builtin_amdgcn_wave_barrier();
    f32x16 d = (f32x16)(0.f);
#pragma unroll
    for (int ks = 0; ks < 4; ++ks) {
      bf16x8 a = *reinterpret_cast<const bf16x8*>(kvT + r * PQ_LD + ks * 16 + h * 8);
      bf16x8 b = *reinterpret_cast<const bf16x8*>(&pq[wid][r * PQ_LD + ks * 16 + h * 8]);
      d = __builtin_amdgcn_mfma_f32_32x32x16_bf16(a, b, d, 0, 0, 0);
    }
    __builtin_amdgcn_wave_barrier();
    u16* orow = o + (size_t)(s0 + sl + r) * D_ + head * 32;
#pragma unroll
    for (int g = 0; g < 4; ++g) {
      bf16x4 v;
#pragma unroll
      for (int j = 0; j < 4; ++j) v[j] = (short)f2bf(rbf(d[4 * g + j]) / den);
      *reinterpret_cast<bf16x4*>(orow + 8 * g + 4 * h) = v;
    }
  }
}

extern "C" void kernel_launch(void* const* d_in, const int* in_sizes, int n_in, void* d_out, int out_size,
                              void* d_ws, size_t ws_size, hipStream_t stream) {
  const u16* x = (const u16*)d_in[0];
  const u16* wqkv = (const u16*)d_in[1];
  const u16* wphi = (const u16*)d_in[2];
  const u16* wo = (const u16*)d_in[3];
  const u16* w1 = (const u16*)d_in[5];
  const u16* w2 = (const u16*)d_in[6];
  const u16* w3 = (const u16*)d_in[7];
  const u16* w4 = (const u16*)d_in[8];
  const u16* gate_w = (const u16*)d_in[9];
  char* ws = (char*)d_ws;
  u16* qkv = (u16*)(ws + WS_QKV);
  u16* hbuf = (u16*)(ws + WS_H);
  u16* x1 = (u16*)(ws + WS_X1);
  u16* h3 = (u16*)(ws + WS_H3);
  u16* o = (u16*)(ws + WS_O);
  float* xstat = (float*)(ws + WS_XSTAT);
  float* gate = (float*)(ws + WS_GATE);
  float* cs1 = (float*)(ws + WS_CS1);
  float* cs3 = (float*)(ws + WS_CS3);
  float* pstat = (float*)(ws + WS_PSTAT);
  float* kvp = (float*)(ws + WS_KVP);
  float* zp = (float*)(ws + WS_ZP);

  constexpr int LDS1 = NSTAGE * G1::STAGE_BYTES + 2048;
  constexpr int LDS2 = NSTAGE * G2::STAGE_BYTES;
  hipFuncSetAttribute((const void*)gemm1_kernel, hipFuncAttributeMaxDynamicSharedMemorySize, LDS1);
  hipFuncSetAttribute((const void*)gemm3_kernel, hipFuncAttributeMaxDynamicSharedMemorySize, LDS1);
  hipFuncSetAttribute((const void*)gemm2_kernel, hipFuncAttributeMaxDynamicSharedMemorySize, LDS2);
  hipFuncSetAttribute((const void*)gemm4_kernel, hipFuncAttributeMaxDynamicSharedMemorySize, LDS2);

  prep_kernel<<<S_ / 4 + 72 + 2, 256, 0, stream>>>(x, gate_w, w1, w3, xstat, gate, cs1, cs3, hbuf);
  gemm1_kernel<<<256, 512, LDS1, stream>>>(x, wqkv, w1, xstat, cs1, qkv, hbuf);
  attn_kv_kernel<<<256, 256, 0, stream>>>(qkv, wphi, kvp, zp);
  gemm2_kernel<<<256, 256, LDS2, stream>>>(hbuf, w2, x, x1, pstat);
  attn_out_kernel<<<256, 256, 0, stream>>>(qkv, wphi, kvp, zp, o);
  gemm3_kernel<<<224, 512, LDS1, stream>>>(x1, w3, pstat, cs3, h3);
  gemm4_kernel<<<256, 256, LDS2, stream>>>(o, wo, h3, w4, x1, gate, (u16*)d_out);
}
```

```cpp
#include <hip/hip_runtime.h>
#include <hip/hip_bf16.h>
#include <stdint.h>

constexpr int S_ = 2048, D_ = 1024, H_ = 32, DH = 32, F_ = 3584;
constexpr int NQKV = 3 * D_;

typedef unsigned short u16;
typedef __attribute__((ext_vector_type(8))) short bf16x8;
typedef __attribute__((ext_vector_type(4))) short bf16x4;
typedef __attribute__((ext_vector_type(16))) float f32x16;
typedef __attribute__((ext_vector_type(4))) float f32x4;
typedef __attribute__((ext_vector_type(4))) unsigned int u32x4;
typedef __attribute__((ext_vector_type(2))) unsigned int u32x2;

__device__ __forceinline__ float bf2f(u16 v) { return __uint_as_float(((unsigned)v) << 16); }
__device__ __forceinline__ u16 f2bf(float f) {
  __hip_bfloat16 b = __float2bfloat16(f);
  return *reinterpret_cast<u16*>(&b);
}
__device__ __forceinline__ float rbf(float f) { return bf2f(f2bf(f)); }
__device__ __forceinline__ float gelu_f(float x) {
  const float k0 = 0.7978845608028654f, k1 = 0.044715f;
  float inner = k0 * (x + k1 * x * x * x);
  return x / (1.0f + __expf(-2.0f * inner));
}

constexpr size_t WS_QKV   = 0;
constexpr size_t WS_H     = WS_QKV + (size_t)S_ * NQKV * 2;
constexpr size_t WS_X1    = WS_H + (size_t)(S_ + 4) * D_ * 2;
constexpr size_t WS_H3    = WS_X1 + (size_t)S_ * D_ * 2;
constexpr size_t WS_O     = WS_H3 + (size_t)S_ * F_ * 2;
constexpr size_t WS_XSTAT = WS_O + (size_t)S_ * D_ * 2;
constexpr size_t WS_GATE  = WS_XSTAT + (size_t)S_ * 2 * 4;
constexpr size_t WS_CS1   = WS_GATE + (size_t)S_ * 4;
constexpr size_t WS_CS3   = WS_CS1 + (size_t)D_ * 4;
constexpr size_t WS_PSTAT = WS_CS3 + (size_t)F_ * 4;
constexpr size_t WS_KVP   = WS_PSTAT + (size_t)S_ * 16 * 2 * 4;
constexpr size_t WS_ZP    = WS_KVP + (size_t)H_ * 16 * 64 * 32 * 4;
constexpr size_t WS_END   = WS_ZP + (size_t)H_ * 8 * 64 * 4;

__global__ __launch_bounds__(256) void prep_kernel(const u16* __restrict__ x, const u16* __restrict__ gate_w,
                                                   const u16* __restrict__ w1, const u16* __restrict__ w3,
                                                   float* __restrict__ xstat, float* __restrict__ gate,
                                                   float* __restrict__ cs1, float* __restrict__ cs3,
                                                   u16* __restrict__ hbuf) {
  __shared__ float red[32][65];
  const int tid = threadIdx.x, lane = tid & 63, wid = tid >> 6;
  int b = blockIdx.x;
  if (b < S_ / 4) {
    int row = b * 4 + wid;
    const u16* xr = x + (size_t)row * D_;
    float s = 0.f, ss = 0.f, gd = 0.f;
#pragma unroll
    for (int i = 0; i < 2; ++i) {
      int c = (i * 64 + lane) * 8;
      bf16x8 v = *reinterpret_cast<const bf16x8*>(xr + c);
      bf16x8 g = *reinterpret_cast<const bf16x8*>(gate_w + c);
#pragma unroll
      for (int j = 0; j < 8; ++j) {
        float f = bf2f((u16)v[j]);
        s += f; ss += f * f;
        gd += rbf(f * bf2f((u16)g[j]));
      }
    }
#pragma unroll
    for (int o = 32; o > 0; o >>= 1) {
      s += __shfl_xor(s, o); ss += __shfl_xor(ss, o); gd += __shfl_xor(gd, o);
    }
    if (lane == 0) {
      float mean = s * (1.0f / D_);
      float var = ss * (1.0f / D_) - mean * mean;
      var = fmaxf(var, 0.f);
      xstat[row * 2] = mean;
      xstat[row * 2 + 1] = rsqrtf(var + 1e-5f);
      float gs = rbf(gd);
      gate[row] = rbf(1.0f / (1.0f + __expf(-gs)));
    }
    return;
  }
  b -= S_ / 4;
  if (b < 16 + 56) {
    const u16* w; float* out; int ldw; int n0;
    if (b < 16) { w = w1; out = cs1; ldw = D_; n0 = b * 64; }
    else { w = w3; out = cs3; ldw = F_; n0 = (b - 16) * 64; }
    int ch = tid & 7, kg = tid >> 3;
    float acc[8] = {0, 0, 0, 0, 0, 0, 0, 0};
    for (int r = 0; r < 32; ++r) {
      bf16x8 v = *reinterpret_cast<const bf16x8*>(w + (size_t)(kg * 32 + r) * ldw + n0 + ch * 8);
#pragma unroll
      for (int j = 0; j < 8; ++j) acc[j] += bf2f((u16)v[j]);
    }
#pragma unroll
    for (int j = 0; j < 8; ++j) red[kg][ch * 8 + j] = acc[j];
    __syncthreads();
    if (tid < 64) {
      float s = 0.f;
      for (int k = 0; k < 32; ++k) s += red[k][tid];
      out[n0 + tid] = s;
    }
    return;
  }
  b -= 72;
  {
    int idx = tid * 8;
    u32x4 z = {0, 0, 0, 0};
    if (b == 0) *reinterpret_cast<u32x4*>(hbuf + idx) = z;
    else *reinterpret_cast<u32x4*>(hbuf + (size_t)(S_ + 2) * D_ + idx) = z;
  }
}

constexpr int BK = 64;
constexpr int NSTAGE = 3;

template <int BN> __device__ __forceinline__ int bswz(int row) {
  if constexpr (BN == 128) return ((row & 3) << 2) | ((row >> 2) & 3);
  else return ((row >> 1) & 1) << 2;
}

template <int BM, int BN, int WM, int WN>
struct Gemm {
  static constexpr int NW = WM * WN;
  static constexpr int TM = BM / WM / 32;
  static constexpr int TN = BN / WN / 32;
  static constexpr int A_BYTES = BM * BK * 2;
  static constexpr int B_BYTES = BK * BN * 2;
  static constexpr int STAGE_BYTES = A_BYTES + B_BYTES;
  static constexpr int APW = BM / 8 / NW;
  static constexpr int BPW = BN / 8 / NW;
  static constexpr int LPS = APW + BPW;

  template <bool SHIFT>
  static __device__ __forceinline__ void stage(char* lds, const u16* A, int lda, int m0, const u16* B, int ldb,
                                               int n0, int k0, int wid, int lane) {
    int shift = 0;
    if constexpr (SHIFT) {
      int q = k0 >> 8;
      shift = (q == 0) ? -1 : (q == 1) ? 1 : (q == 2) ? -2 : 2;
    }
#pragma unroll
    for (int j = 0; j < APW; ++j) {
      int ia = wid * APW + j;
      int row = 8 * ia + (lane >> 3);
      int c = (lane & 7) ^ ((row >> 1) & 7);
      const u16* src = A + (long)(m0 + row + shift) * lda + k0 + c * 8;
      __builtin_amdgcn_global_load_lds((const unsigned*)src, (__attribute__((address_space(3))) unsigned*)(lds + ia * 1024), 16, 0, 0);
    }
#pragma unroll
    for (int j = 0; j < BPW; ++j) {
      int ib = wid * BPW + j;
      constexpr int CPR = BN / 8;
      constexpr int RPI = 64 / CPR;
      int row = RPI * ib + lane / CPR;
      int c = (lane % CPR) ^ bswz<BN>(row);
      const u16* src = B + (long)(k0 + row) * ldb + n0 + c * 8;
      __builtin_amdgcn_global_load_lds((const unsigned*)src, (__attribute__((address_space(3))) unsigned*)(lds + A_BYTES + ib * 1024), 16, 0, 0);
    }
  }

  struct Frag { bf16x8 a[TM]; bf16x4 lo[TN], hi[TN]; };
  static __device__ __forceinline__ void ldfrag(Frag& f, unsigned sb, unsigned aoff, unsigned boff, int s) {
    const unsigned aa = sb + (aoff ^ (unsigned)(s << 5));

#pragma unroll
    for (int tm = 0; tm < TM; ++tm)
      asm volatile("ds_read_b128 %0, %1 offset:%2" : "=v"(f.a[tm]) : "v"(aa), "n"(tm * 32 * 128));
#pragma unroll
    for (int tn = 0; tn < TN; ++tn) {
      asm volatile("ds_read_b64_tr_b16 %0, %1 offset:%2" : "=v"(f.lo[tn]) : "v"(sb + (boff ^ (unsigned)(tn * 64))), "n"(s * 16 * BN * 2));
      asm volatile("ds_read_b64_tr_b16 %0, %1 offset:%2" : "=v"(f.hi[tn]) : "v"(sb + (boff ^ BHI ^ (unsigned)(tn * 64))), "n"(s * 16 * BN * 2 + 4 * BN * 2));
    }
  }
  static __device__ __forceinline__ void waitfrag(Frag& f, int n) {
    if (n == 0) {
      if constexpr (TM == 2 && TN == 1) asm volatile("s_waitcnt lgkmcnt(0)" : "+v"(f.a[0]), "+v"(f.a[1]), "+v"(f.lo[0]), "+v"(f.hi[0]));
      else asm volatile("s_waitcnt lgkmcnt(0)" : "+v"(f.a[0]), "+v"(f.a[1]), "+v"(f.lo[0]), "+v"(f.hi[0]), "+v"(f.lo[1]), "+v"(f.hi[1]));
    } else {
      if constexpr (TM == 2 && TN == 1) asm volatile("s_waitcnt lgkmcnt(4)" : "+v"(f.a[0]), "+v"(f.a[1]), "+v"(f.lo[0]), "+v"(f.hi[0]));
      else asm volatile("s_waitcnt lgkmcnt(6)" : "+v"(f.a[0]), "+v"(f.a[1]), "+v"(f.lo[0]), "+v"(f.hi[0]), "+v"(f.lo[1]), "+v"(f.hi[1]));
    }
  }
  static __device__ __forceinline__ void mma(Frag& f, f32x16 (&acc)[TM][TN]) {
#pragma unroll
    for (int tm = 0; tm < TM; ++tm)
#pragma unroll
      for (int tn = 0; tn < TN; ++tn)
        acc[tm][tn] = __builtin_amdgcn_mfma_f32_32x32x16_bf16(__builtin_shufflevector(f.lo[tn], f.hi[tn], 0, 1, 2, 3, 4, 5, 6, 7), f.a[tm], acc[tm][tn], 0, 0, 0);
  }
  static constexpr unsigned BHI = (BN == 128) ? (1u << 4) : 0u;

  static __device__ __forceinline__ void compute(unsigned stage_base, unsigned aoff, unsigned boff, f32x16 (&acc)[TM][TN]) {
    Frag f0, f1;
    ldfrag(f0, stage_base, aoff, boff, 0);
    ldfrag(f1, stage_base, aoff, boff, 1);
    waitfrag(f0, 1);
    mma(f0, acc);
    ldfrag(f0, stage_base, aoff, boff, 2);
    waitfrag(f1, 1);
    mma(f1, acc);
    ldfrag(f1, stage_base, aoff, boff, 3);
    waitfrag(f0, 1);
    mma(f0, acc);
    waitfrag(f1, 0);
    mma(f1, acc);
  }

  template <bool SHIFT>
  static __device__ __forceinline__ void mainloop(char* lds, f32x16 (&acc)[TM][TN], const u16* A, int lda, int m0,
                                                  const u16* B, int ldb, int n0, int K, int wid, int lane) {
    const int wm = wid / WN, wn = wid % WN;
    const int nt = K / BK;
    const unsigned lbase = (unsigned)(size_t)lds;
    unsigned aoff, boff;
    {
      const int r = lane & 31, h = lane >> 5;
      const int blk = (lane >> 4) & 1, q = (lane & 15) >> 2, p = lane & 3;
      int row = wm * (TM * 32) + r;
      aoff = row * 128 + ((h ^ ((row >> 1) & 7)) << 4);
      int krow = 8 * h + q;
      int ch = (wn * (TN * 32)) / 8 + 2 * blk + (p >> 1);
      boff = A_BYTES + krow * (BN * 2) + ((ch ^ bswz<BN>(krow)) << 4) + 8 * (p & 1);
    }
    stage<SHIFT>(lds, A, lda, m0, B, ldb, n0, 0, wid, lane);
    stage<SHIFT>(lds + STAGE_BYTES, A, lda, m0, B, ldb, n0, BK, wid, lane);
    int rb = 0, wb = 2;
    for (int t = 0; t < nt; ++t) {
      if (t + 1 < nt) asm volatile("s_waitcnt vmcnt(%0)" ::"n"(LPS) : "memory");
      else asm volatile("s_waitcnt vmcnt(0)" ::: "memory");
      __builtin_amdgcn_s_barrier();
      if (t + 2 < nt) stage<SHIFT>(lds + wb * STAGE_BYTES, A, lda, m0, B, ldb, n0, (t + 2) * BK, wid, lane);
      compute(lbase + rb * STAGE_BYTES, aoff, boff, acc);
      rb = (rb == NSTAGE - 1) ? 0 : rb + 1;
      wb = (wb == NSTAGE - 1) ? 0 : wb + 1;
    }
    __builtin_amdgcn_s_barrier();
  }
};

template <class G, class Fn>
__device__ __forceinline__ void stage_tile(u16* tile, int ld, f32x16 (&acc)[G::TM][G::TN], int wm, int wn, int lane, Fn fn) {
#pragma unroll
  for (int tm = 0; tm < G::TM; ++tm)
#pragma unroll
    for (int tn = 0; tn < G::TN; ++tn) {
      const int row = wm * (G::TM * 32) + tm * 32 + (lane & 31);
#pragma unroll
      for (int g = 0; g < 4; ++g) {
        const int col = wn * (G::TN * 32) + tn * 32 + 8 * g + 4 * (lane >> 5);
        float v[4] = {acc[tm][tn][4 * g], acc[tm][tn][4 * g + 1], acc[tm][tn][4 * g + 2], acc[tm][tn][4 * g + 3]};
        fn(row, col, v);
        bf16x4 o;
#pragma unroll
        for (int j = 0; j < 4; ++j) o[j] = (short)f2bf(v[j]);
        *reinterpret_cast<bf16x4*>(tile + row * ld + col) = o;
      }
    }
}

using G1 = Gemm<256, 128, 4, 2>;
constexpr int EP_LD = 128 + 8;

__global__ __launch_bounds__(512, 2) void gemm1_kernel(const u16* __restrict__ x, const u16* __restrict__ wqkv,
                                                      const u16* __restrict__ w1, const float* __restrict__ xstat,
                                                      const float* __restrict__ cs1, u16* __restrict__ qkv,
                                                      u16* __restrict__ hbuf) {
  extern __shared__ __attribute__((aligned(16))) char lds[];
  const int tid = threadIdx.x, lane = tid & 63, wid = tid >> 6;
  const int bid = blockIdx.x;
  const int mt = bid & 7, ntile = bid >> 3;
  const int m0 = mt * 256;
  const bool isw1 = ntile >= 24;
  const int n0 = isw1 ? (ntile - 24) * 128 : ntile * 128;
  f32x16 acc[G1::TM][G1::TN];
#pragma unroll
  for (int i = 0; i < G1::TM; ++i)
#pragma unroll
    for (int j = 0; j < G1::TN; ++j) acc[i][j] = (f32x16)(0.f);
  G1::mainloop<false>(lds, acc, x, D_, m0, isw1 ? w1 : wqkv, isw1 ? D_ : NQKV, n0, D_, wid, lane);

  u16* tile = reinterpret_cast<u16*>(lds);
  const int wm = wid / 2, wn = wid % 2;
  if (isw1) {
    stage_tile<G1>(tile, EP_LD, acc, wm, wn, lane, [&](int row, int col, float (&v)[4]) {
      const float mean = xstat[(m0 + row) * 2], rstd = xstat[(m0 + row) * 2 + 1];
      const f32x4 cs = *reinterpret_cast<const f32x4*>(cs1 + n0 + col);
#pragma unroll
      for (int j = 0; j < 4; ++j) v[j] = gelu_f(rbf(rstd * (v[j] - mean * cs[j])));
    });
  } else {
    stage_tile<G1>(tile, EP_LD, acc, wm, wn, lane, [&](int, int, float (&)[4]) {});
  }
  __syncthreads();
  u16* dst = isw1 ? (hbuf + (size_t)2 * D_) : qkv;
  const int ldd = isw1 ? D_ : NQKV;
  for (int i = tid; i < 256 * 16; i += 512) {
    int row = i >> 4, ch = i & 15;
    u32x4 v = *reinterpret_cast<const u32x4*>(tile + row * EP_LD + ch * 8);
    *reinterpret_cast<u32x4*>(dst + (size_t)(m0 + row) * ldd + n0 + ch * 8) = v;
  }
}

__device__ __forceinline__ void phi_tile(const u16* __restrict__ rows  , int ld,
                                         const bf16x8 (&wA)[2], int lane, float (&pp)[16], float (&pn)[16]) {
  const int r = lane & 31, h = lane >> 5;
  f32x16 d = (f32x16)(0.f);
#pragma unroll
  for (int ks = 0; ks < 2; ++ks) {
    bf16x8 b = *reinterpret_cast<const bf16x8*>(rows + (size_t)r * ld + ks * 16 + h * 8);
    d = __builtin_amdgcn_mfma_f32_32x32x16_bf16(wA[ks], b, d, 0, 0, 0);
  }
  float mx = -INFINITY, mn = INFINITY;
#pragma unroll
  for (int i = 0; i < 16; ++i) { float v = rbf(d[i]); d[i] = v; mx = fmaxf(mx, v); mn = fminf(mn, v); }
  mx = fmaxf(mx, __shfl_xor(mx, 32)); mn = fminf(mn, __shfl_xor(mn, 32));
  float sp = 0.f, sn = 0.f;
#pragma unroll
  for (int i = 0; i < 16; ++i) {
    pp[i] = __expf(d[i] - mx); pn[i] = __expf(mn - d[i]);
    sp += pp[i]; sn += pn[i];
  }
  sp += __shfl_xor(sp, 32); sn += __shfl_xor(sn, 32);
  float ip = 1.0f / sp, in_ = 1.0f / sn;
#pragma unroll
  for (int i = 0; i < 16; ++i) { pp[i] = rbf(pp[i] * ip); pn[i] = rbf(pn[i] * in_); }
}

__device__ __forceinline__ void load_wphiT(const u16* __restrict__ wphi, int head, int lane, bf16x8 (&wA)[2]) {
  const int r = lane & 31, h = lane >> 5;
#pragma unroll
  for (int ks = 0; ks < 2; ++ks)
#pragma unroll
    for (int j = 0; j < 8; ++j) wA[ks][j] = (short)wphi[(size_t)head * 1024 + (16 * ks + 8 * h + j) * 32 + r];
}

constexpr int PHI_LD = 256 + 8;
__device__ __forceinline__ void attn_kv_block(char* lds, int bidx, const u16* __restrict__ qkv, const u16* __restrict__ wphi,
                                              float* __restrict__ kvp, float* __restrict__ zp) {
  u16* phi = reinterpret_cast<u16*>(lds);
  u16* vT = phi + 64 * PHI_LD;
  const int tid = threadIdx.x, lane = tid & 63, wid = tid >> 6;
  const int head = bidx >> 3, sp = bidx & 7;
  const int s0 = sp * 256;
  bf16x8 wA[2];
  load_wphiT(wphi, head, lane, wA);
  const int h = lane >> 5;
#pragma unroll
  for (int t = 0; t < 2; ++t) {
    int sl = wid * 64 + t * 32;
    float pp[16], pn[16];
    phi_tile(qkv + (size_t)(s0 + sl) * NQKV + D_ + head * 32, NQKV, wA, lane, pp, pn);
#pragma unroll
    for (int i = 0; i < 16; ++i) {
      int f = (i & 3) + 8 * (i >> 2) + 4 * h;
      phi[f * PHI_LD + sl + (lane & 31)] = f2bf(pp[i]);
      phi[(32 + f) * PHI_LD + sl + (lane & 31)] = f2bf(pn[i]);
    }
  }
  {
    const u16* vr = qkv + (size_t)(s0 + tid) * NQKV + 2 * D_ + head * 32;
#pragma unroll
    for (int c = 0; c < 4; ++c) {
      bf16x8 v = *reinterpret_cast<const bf16x8*>(vr + c * 8);
#pragma unroll
      for (int j = 0; j < 8; ++j) vT[(c * 8 + j) * PHI_LD + tid] = (u16)v[j];
    }
  }
  __syncthreads();
  {
    const int ft = wid & 1, sh = wid >> 1;
    const int r = lane & 31;
    f32x16 d = (f32x16)(0.f);
#pragma unroll
    for (int ks = 0; ks < 8; ++ks) {
      int sb = sh * 128 + ks * 16 + h * 8;
      bf16x8 a = *reinterpret_cast<const bf16x8*>(phi + (ft * 32 + r) * PHI_LD + sb);
      bf16x8 b = *reinterpret_cast<const bf16x8*>(vT + r * PHI_LD + sb);
      d = __builtin_amdgcn_mfma_f32_32x32x16_bf16(a, b, d, 0, 0, 0);
    }
    float* dst = kvp + ((size_t)(head * 16 + sp * 2 + sh) * 64 + ft * 32) * 32;
#pragma unroll
    for (int i = 0; i < 16; ++i) {
      int f = (i & 3) + 8 * (i >> 2) + 4 * h;
      dst[f * 32 + r] = d[i];
    }
  }
  if (tid < 64) {
    float s = 0.f;
    for (int i = 0; i < 256; i += 8) {
      bf16x8 v = *reinterpret_cast<const bf16x8*>(phi + tid * PHI_LD + i);
#pragma unroll
      for (int j = 0; j < 8; ++j) s += bf2f((u16)v[j]);
    }
    zp[(head * 8 + sp) * 64 + tid] = s;
  }
}

constexpr int PQ_LD = 64 + 8;
__device__ __forceinline__ void attn_out_block(char* lds, int bidx, const u16* __restrict__ qkv, const u16* __restrict__ wphi,
                                               const float* __restrict__ kvp, const float* __restrict__ zp,
                                               u16* __restrict__ o) {
  u16* kvT = reinterpret_cast<u16*>(lds);
  float* zs = reinterpret_cast<float*>(lds + 32 * PQ_LD * 2);
  u16 (*pq)[32 * PQ_LD] = reinterpret_cast<u16 (*)[32 * PQ_LD]>(lds + 32 * PQ_LD * 2 + 256);
  const int tid = threadIdx.x, lane = tid & 63, wid = tid >> 6;
  const int head = bidx >> 3, sb = bidx & 7;
  const int s0 = sb * 256;
  for (int i = tid; i < 64 * 32; i += 256) {
    float s = 0.f;
    for (int p = 0; p < 16; ++p) s += kvp[(size_t)(head * 16 + p) * 2048 + i];
    int f = i >> 5, d = i & 31;
    kvT[d * PQ_LD + f] = f2bf(s);
  }
  if (tid < 64) {
    float s = 0.f;
    for (int p = 0; p < 8; ++p) s += zp[(head * 8 + p) * 64 + tid];
    zs[tid] = rbf(s);
  }
  bf16x8 wA[2];
  load_wphiT(wphi, head, lane, wA);
  __syncthreads();
  const int h = lane >> 5, r = lane & 31;
#pragma unroll
  for (int t = 0; t < 2; ++t) {
    int sl = wid * 64 + t * 32;
    float pp[16], pn[16];
    phi_tile(qkv + (size_t)(s0 + sl) * NQKV + head * 32, NQKV, wA, lane, pp, pn);
    float den = 0.f;
#pragma unroll
    for (int g = 0; g < 4; ++g) {
      int f = 8 * g + 4 * h;
      bf16x4 a, b;
#pragma unroll
      for (int j = 0; j < 4; ++j) {
        a[j] = (short)f2bf(pp[4 * g + j]); b[j] = (short)f2bf(pn[4 * g + j]);
        den += pp[4 * g + j] * zs[f + j] + pn[4 * g + j] * zs[32 + f + j];
      }
      *reinterpret_cast<bf16x4*>(&pq[wid][r * PQ_LD + f]) = a;
      *reinterpret_cast<bf16x4*>(&pq[wid][r * PQ_LD + 32 + f]) = b;
    }
    den += __shfl_xor(den, 32);
    den = rbf(rbf(den) + 1e-6f);
    __builtin_amdgcn_s_waitcnt(0xC07F);
    __builtin_amdgcn_wave_barrier();
    f32x16 d = (f32x16)(0.f);
#pragma unroll
    for (int ks = 0; ks < 4; ++ks) {
      bf16x8 a = *reinterpret_cast<const bf16x8*>(kvT + r * PQ_LD + ks * 16 + h * 8);
      bf16x8 b = *reinterpret_cast<const bf16x8*>(&pq[wid][r * PQ_LD + ks * 16 + h * 8]);
      d = __builtin_amdgcn_mfma_f32_32x32x16_bf16(a, b, d, 0, 0, 0);
    }
    __builtin_amdgcn_wave_barrier();
    u16* orow = o + (size_t)(s0 + sl + r) * D_ + head * 32;
#pragma unroll
    for (int g = 0; g < 4; ++g) {
      bf16x4 v;
#pragma unroll
      for (int j = 0; j < 4; ++j) v[j] = (short)f2bf(rbf(d[4 * g + j]) / den);
      *reinterpret_cast<bf16x4*>(orow + 8 * g + 4 * h) = v;
    }
  }
}

using G2 = Gemm<128, 64, 2, 2>;
constexpr int EP2_LD = 64 + 8;

__global__ __launch_bounds__(256, 2) void gemm2_kernel(const u16* __restrict__ hbuf, const u16* __restrict__ w2,
                                                      const u16* __restrict__ x, u16* __restrict__ x1,
                                                      float* __restrict__ pstat, const u16* __restrict__ qkv,
                                                      const u16* __restrict__ wphi, float* __restrict__ kvp,
                                                      float* __restrict__ zp) {
  extern __shared__ __attribute__((aligned(16))) char lds[];
  if (blockIdx.x >= 256) { attn_kv_block(lds, blockIdx.x - 256, qkv, wphi, kvp, zp); return; }
  const int tid = threadIdx.x, lane = tid & 63, wid = tid >> 6;
  const int bid = blockIdx.x;
  const int mt = bid & 15, ntile = bid >> 4;
  const int m0 = mt * 128, n0 = ntile * 64;
  f32x16 acc[G2::TM][G2::TN];
#pragma unroll
  for (int i = 0; i < G2::TM; ++i)
#pragma unroll
    for (int j = 0; j < G2::TN; ++j) acc[i][j] = (f32x16)(0.f);
  G2::mainloop<true>(lds, acc, hbuf + (size_t)2 * D_, D_, m0, w2, D_, n0, D_, wid, lane);
  u16* tile = reinterpret_cast<u16*>(lds);
  const int wm = wid / 2, wn = wid % 2;
  stage_tile<G2>(tile, EP2_LD, acc, wm, wn, lane, [&](int, int, float (&)[4]) {});
  __syncthreads();
  for (int i = tid; i < 128 * 8; i += 256) {
    int row = i >> 3, ch = i & 7;
    bf16x8 hv = *reinterpret_cast<const bf16x8*>(tile + row * EP2_LD + ch * 8);
    bf16x8 xv = *reinterpret_cast<const bf16x8*>(x + (size_t)(m0 + row) * D_ + n0 + ch * 8);
    bf16x8 ov;
    float s = 0.f, ss = 0.f;
#pragma unroll
    for (int j = 0; j < 8; ++j) {
      u16 o = f2bf(bf2f((u16)xv[j]) + bf2f((u16)hv[j]));
      float f = bf2f(o);
      s += f; ss += f * f;
      ov[j] = (short)o;
    }
    *reinterpret_cast<bf16x8*>(x1 + (size_t)(m0 + row) * D_ + n0 + ch * 8) = ov;
#pragma unroll
    for (int o = 4; o > 0; o >>= 1) { s += __shfl_xor(s, o); ss += __shfl_xor(ss, o); }
    if (ch == 0) {
      pstat[((m0 + row) * 16 + ntile) * 2] = s;
      pstat[((m0 + row) * 16 + ntile) * 2 + 1] = ss;
    }
  }
}

__global__ __launch_bounds__(512, 2) void gemm3_kernel(const u16* __restrict__ x1, const u16* __restrict__ w3,
                                                      const float* __restrict__ pstat, const float* __restrict__ cs3,
                                                      u16* __restrict__ h3, const u16* __restrict__ qkv,
                                                      const u16* __restrict__ wphi, const float* __restrict__ kvp,
                                                      const float* __restrict__ zp, u16* __restrict__ o) {
  extern __shared__ __attribute__((aligned(16))) char lds[];
  if (blockIdx.x >= 224) {
    if (threadIdx.x < 256) attn_out_block(lds, blockIdx.x - 224, qkv, wphi, kvp, zp, o);
    return;
  }
  const int tid = threadIdx.x, lane = tid & 63, wid = tid >> 6;
  const int bid = blockIdx.x;
  const int mt = bid & 7, ntile = bid >> 3;
  const int m0 = mt * 256, n0 = ntile * 128;
  float* rstat = reinterpret_cast<float*>(lds + NSTAGE * G1::STAGE_BYTES);
  if (tid < 256) {
    float s = 0.f, ss = 0.f;
    const float* p = pstat + (size_t)(m0 + tid) * 32;
    for (int i = 0; i < 16; ++i) { s += p[2 * i]; ss += p[2 * i + 1]; }
    float mean = s * (1.0f / D_);
    float var = fmaxf(ss * (1.0f / D_) - mean * mean, 0.f);
    rstat[tid * 2] = mean;
    rstat[tid * 2 + 1] = rsqrtf(var + 1e-5f);
  }
  f32x16 acc[G1::TM][G1::TN];
#pragma unroll
  for (int i = 0; i < G1::TM; ++i)
#pragma unroll
    for (int j = 0; j < G1::TN; ++j) acc[i][j] = (f32x16)(0.f);
  G1::mainloop<false>(lds, acc, x1, D_, m0, w3, F_, n0, D_, wid, lane);
  u16* tile = reinterpret_cast<u16*>(lds);
  const int wm = wid / 2, wn = wid % 2;
  stage_tile<G1>(tile, EP_LD, acc, wm, wn, lane, [&](int row, int col, float (&v)[4]) {
    const float mean = rstat[row * 2], rstd = rstat[row * 2 + 1];
    const f32x4 cs = *reinterpret_cast<const f32x4*>(cs3 + n0 + col);
#pragma unroll
    for (int j = 0; j < 4; ++j) v[j] = gelu_f(rbf(rstd * (v[j] - mean * cs[j])));
  });
  __syncthreads();
  for (int i = tid; i < 256 * 16; i += 512) {
    int row = i >> 4, ch = i & 15;
    u32x4 v = *reinterpret_cast<const u32x4*>(tile + row * EP_LD + ch * 8);
    *reinterpret_cast<u32x4*>(h3 + (size_t)(m0 + row) * F_ + n0 + ch * 8) = v;
  }
}

__global__ __launch_bounds__(256, 2) void gemm4_kernel(const u16* __restrict__ o, const u16* __restrict__ wo,
                                                      const u16* __restrict__ h3, const u16* __restrict__ w4,
                                                      const u16* __restrict__ x1, const float* __restrict__ gate,
                                                      u16* __restrict__ out) {
  extern __shared__ __attribute__((aligned(16))) char lds[];
  const int tid = threadIdx.x, lane = tid & 63, wid = tid >> 6;
  const int bid = blockIdx.x;
  const int mt = bid & 15, ntile = bid >> 4;
  const int m0 = mt * 128, n0 = ntile * 64;
  f32x16 acc[G2::TM][G2::TN], acc2[G2::TM][G2::TN];
#pragma unroll
  for (int i = 0; i < G2::TM; ++i)
#pragma unroll
    for (int j = 0; j < G2::TN; ++j) { acc[i][j] = (f32x16)(0.f); acc2[i][j] = (f32x16)(0.f); }
  G2::mainloop<false>(lds, acc, o, D_, m0, wo, D_, n0, D_, wid, lane);
  G2::mainloop<false>(lds, acc2, h3, F_, m0, w4, D_, n0, F_, wid, lane);
  u16* tileA = reinterpret_cast<u16*>(lds);
  u16* tileB = tileA + 128 * EP2_LD;
  const int wm = wid / 2, wn = wid % 2;
  stage_tile<G2>(tileA, EP2_LD, acc, wm, wn, lane, [&](int, int, float (&)[4]) {});
  stage_tile<G2>(tileB, EP2_LD, acc2, wm, wn, lane, [&](int, int, float (&)[4]) {});
  __syncthreads();
  for (int i = tid; i < 128 * 8; i += 256) {
    int row = i >> 3, ch = i & 7;
    bf16x8 av = *reinterpret_cast<const bf16x8*>(tileA + row * EP2_LD + ch * 8);
    bf16x8 hv = *reinterpret_cast<const bf16x8*>(tileB + row * EP2_LD + ch * 8);
    bf16x8 xv = *reinterpret_cast<const bf16x8*>(x1 + (size_t)(m0 + row) * D_ + n0 + ch * 8);
    float g = gate[m0 + row];
    float omg = rbf(1.0f - g);
    bf16x8 ov;
#pragma unroll
    for (int j = 0; j < 8; ++j) {
      float bb = rbf(bf2f((u16)xv[j]) + bf2f((u16)hv[j]));
      float t1 = rbf(g * bf2f((u16)av[j]));
      float t2 = rbf(omg * bb);
      ov[j] = (short)f2bf(t1 + t2);
    }
    *reinterpret_cast<bf16x8*>(out + (size_t)(m0 + row) * D_ + n0 + ch * 8) = ov;
  }
}

extern "C" void kernel_launch(void* const* d_in, const int* in_sizes, int n_in, void* d_out, int out_size,
                              void* d_ws, size_t ws_size, hipStream_t stream) {
  const u16* x = (const u16*)d_in[0];
  const u16* wqkv = (const u16*)d_in[1];
  const u16* wphi = (const u16*)d_in[2];
  const u16* wo = (const u16*)d_in[3];
  const u16* w1 = (const u16*)d_in[5];
  const u16* w2 = (const u16*)d_in[6];
  const u16* w3 = (const u16*)d_in[7];
  const u16* w4 = (const u16*)d_in[8];
  const u16* gate_w = (const u16*)d_in[9];
  char* ws = (char*)d_ws;
  u16* qkv = (u16*)(ws + WS_QKV);
  u16* hbuf = (u16*)(ws + WS_H);
  u16* x1 = (u16*)(ws + WS_X1);
  u16* h3 = (u16*)(ws + WS_H3);
  u16* o = (u16*)(ws + WS_O);
  float* xstat = (float*)(ws + WS_XSTAT);
  float* gate = (float*)(ws + WS_GATE);
  float* cs1 = (float*)(ws + WS_CS1);
  float* cs3 = (float*)(ws + WS_CS3);
  float* pstat = (float*)(ws + WS_PSTAT);
  float* kvp = (float*)(ws + WS_KVP);
  float* zp = (float*)(ws + WS_ZP);

  constexpr int LDS1 = NSTAGE * G1::STAGE_BYTES + 2048;
  constexpr int LDS2 = NSTAGE * G2::STAGE_BYTES;
  hipFuncSetAttribute((const void*)gemm1_kernel, hipFuncAttributeMaxDynamicSharedMemorySize, LDS1);
  hipFuncSetAttribute((const void*)gemm3_kernel, hipFuncAttributeMaxDynamicSharedMemorySize, LDS1);
  hipFuncSetAttribute((const void*)gemm2_kernel, hipFuncAttributeMaxDynamicSharedMemorySize, LDS2);
  hipFuncSetAttribute((const void*)gemm4_kernel, hipFuncAttributeMaxDynamicSharedMemorySize, LDS2);

  prep_kernel<<<S_ / 4 + 72 + 2, 256, 0, stream>>>(x, gate_w, w1, w3, xstat, gate, cs1, cs3, hbuf);
  gemm1_kernel<<<256, 512, LDS1, stream>>>(x, wqkv, w1, xstat, cs1, qkv, hbuf);
  gemm2_kernel<<<512, 256, LDS2, stream>>>(hbuf, w2, x, x1, pstat, qkv, wphi, kvp, zp);
  gemm3_kernel<<<224 + 256, 512, LDS1, stream>>>(x1, w3, pstat, cs3, h3, qkv, wphi, kvp, zp, o);
  gemm4_kernel<<<256, 256, LDS2, stream>>>(o, wo, h3, w4, x1, gate, (u16*)d_out);
}
```
